```python
import torch

def _softmax(x, axis=-1):
    return torch.softmax(x, dim=axis)
def _gelu(x):
    return torch.nn.functional.gelu(x, approximate="tanh")
def _silu(x):
    return torch.nn.functional.silu(x)
def _sigmoid(x):
    return torch.sigmoid(x)
def _layernorm(x, axis=-1, eps=1e-5):
    m = x.mean(dim=axis, keepdim=True)
    v = x.var(dim=axis, keepdim=True, unbiased=False)
    return (x - m) / torch.sqrt(v + eps)
def _scatter_add(tgt, idx, src, axis=0):
    return tgt.clone().index_add_(axis, idx, src)
def _segment_sum(src, seg, n):
    out = torch.zeros((n,) + src.shape[1:], dtype=src.dtype)
    return out.index_add_(0, seg, src)
def _pad(x, pw):
    flat = tuple(v for pair in reversed(pw) for v in pair)
    return torch.nn.functional.pad(x, flat)

kernel_name = 'recurrent_scan__ssm_conv_h3__glu_gated_reglu__prompt'

def setup_inputs():

    _w = (lambda a: (lambda t: t.to(torch.bfloat16) if t.is_floating_point() else t)(torch.as_tensor(a)))
    def _A_setup():
        B, S, D = 1, 2048, 448
        ks = 4
        return {
            "q": _w((lambda *s: torch.randn(*s, dtype=torch.bfloat16) * 0.05)(B, S, D)),
            "k": _w((lambda *s: torch.randn(*s, dtype=torch.bfloat16) * 0.8)(B, S, D)),
            "v": _w((lambda *s: torch.randn(*s, dtype=torch.bfloat16) * 0.8)(B, S, D)),
            "shift_k": _w((lambda *s: torch.randn(*s, dtype=torch.bfloat16) * 0.8)(D, ks) * 0.5),
            "ssm_k": _w((lambda *s: torch.randn(*s, dtype=torch.bfloat16) * 0.8)(D, S) * (1.0 / S ** 0.5)),
            "ssm_D": _w((lambda *s: torch.randn(*s, dtype=torch.bfloat16) * 0.8)(D)),
        }
    _A_in = _A_setup()

    def _B_setup():
        B, S, D, F = 1, 2048, 448, 1792
        return {
            "x": _w((lambda *s: torch.randn(*s, dtype=torch.bfloat16) * 0.05)(B, S, D)),
            "W_gate": _w((lambda *s: torch.randn(*s, dtype=torch.bfloat16) * 0.2)(D, F) * (2.0 / D) ** 0.5),
            "W_up": _w((lambda *s: torch.randn(*s, dtype=torch.bfloat16) * 0.2)(D, F) * (2.0 / D) ** 0.5),
            "W_down": _w((lambda *s: torch.randn(*s, dtype=torch.bfloat16) * 0.2)(F, D) * (1.0 / F) ** 0.5),
        }
    _B_in = _B_setup()

    return {
        "A_q": _A_in["q"],
        "A_k": _A_in["k"],
        "A_v": _A_in["v"],
        "A_shift_k": _A_in["shift_k"],
        "A_ssm_k": _A_in["ssm_k"],
        "A_ssm_D": _A_in["ssm_D"],
        "B_x": _B_in["x"],
        "B_W_gate": _B_in["W_gate"],
        "B_W_up": _B_in["W_up"],
        "B_W_down": _B_in["W_down"],
    }

def reference(A_q, A_k, A_v, A_shift_k, A_ssm_k, A_ssm_D, B_x, B_W_gate, B_W_up, B_W_down):
    def _A_ref(q, k, v, shift_k, ssm_k, ssm_D):
        B, S, D = q.shape
        ks = shift_k.shape[-1]
        k_pad = _pad(k, ((0, 0), (ks - 1, 0), (0, 0)))
        k_shift = torch.zeros_like(k)
        for t in range(ks):
            k_shift = k_shift + k_pad[:, t:t + S, :] * shift_k[:, ks - 1 - t][None, None, :]
        kv = k_shift * v
        L = 2 * S
        kv_pad = _pad(kv, ((0, 0), (0, S), (0, 0)))
        kf_pad = _pad(ssm_k, ((0, 0), (0, S)))
        kv_f = (lambda x, axes=(-2,-1): torch.fft.rfft2(x.float(), dim=axes))(kv_pad[:, None, :, :], axes=(1, 2))
        kf_f = (lambda x, axes=(-2,-1): torch.fft.rfft2(x.float(), dim=axes))(kf_pad[:, None, :], axes=(1, 2))
        kf_f = (lambda x, *d: x.permute(*d))(kf_f, 1, 2, 0)
        y_f = kv_f * kf_f[None, :, :, :]
        y = (lambda x, s, axes=(-2,-1): torch.fft.irfft2(x, s=s, dim=axes).to(torch.bfloat16))(y_f, (1, L), axes=(1, 2))[:, 0, :S, :]
        y = y + kv * ssm_D[None, None, :]
        return q * y

    def _B_ref(x, W_gate, W_up, W_down):
        gate = torch.einsum("bsd,df->bsf", x, W_gate)
        up = torch.einsum("bsd,df->bsf", x, W_up)
        gate = gate * (lambda x, dt: x.to(dt))(gate > 0, gate.dtype)
        h = gate * up
        return torch.einsum("bsf,fd->bsd", h, W_down)

    x = A_q
    xn = _layernorm(x, axis=-1)
    s = _A_ref(xn, A_k, A_v, A_shift_k, A_ssm_k, A_ssm_D)
    g = _silu(_B_ref(xn, B_W_gate, B_W_up, B_W_down))
    return x + s * g
```

```cpp
#include <hip/hip_runtime.h>
#include <stdint.h>

typedef unsigned short u16;
typedef unsigned int u32;
typedef unsigned long long u64;
typedef __attribute__((ext_vector_type(2))) __bf16 bf16x2;
typedef __attribute__((ext_vector_type(8))) __bf16 bf16x8;
typedef __attribute__((ext_vector_type(4))) short s16x4;
typedef __attribute__((ext_vector_type(8))) short s16x8;
typedef __attribute__((ext_vector_type(2))) float f32x2;
typedef __attribute__((ext_vector_type(4))) float f32x4;
typedef __attribute__((ext_vector_type(16))) float f32x16;
typedef __attribute__((ext_vector_type(4))) unsigned u32x4;
typedef __attribute__((ext_vector_type(2))) unsigned u32x2;
#define LAS __attribute__((address_space(3)))
#define GAS __attribute__((address_space(1)))
#define DI __device__ __forceinline__

constexpr int S_ = 2048, D_ = 448, F_ = 1792;
constexpr int XROW = D_ * 2;
constexpr int WROW = F_ * 2;
constexpr u32 WS_H = 0;
constexpr u32 WS_WT = WS_H + 64u * 112 * 1024;
constexpr u32 WS_KVR = WS_WT + 112u * 14 * 1024;
constexpr u32 WS_YT = WS_KVR + 448u * 4096;
constexpr u32 WS_ST = WS_YT + 448u * 4096;
constexpr u32 WS_SP = WS_ST + 2048u * 8;
constexpr u32 WS_CP = WS_SP + 16u * 14 * 128 * 16;
constexpr u32 WS_FP = WS_CP + 14u * 14 * 256 * 8;
constexpr u32 WS_FW = WS_FP + 256;
constexpr u32 WS_FH = WS_FW + 2048;
constexpr u32 WS_FY = WS_FH + 2048;
constexpr u32 WS_END = WS_FY + 2048;
constexpr u32 TAGHI = 0x5EC7A11Du;
constexpr u32 SPIN_CAP = 1u << 16;

DI float bf2f_lo(u32 x) { return __uint_as_float(x << 16); }
DI float bf2f_hi(u32 x) { return __uint_as_float(x & 0xffff0000u); }
DI u32 pack_bf16(float lo, float hi) {
  bf16x2 r;
  r[0] = (__bf16)lo;
  r[1] = (__bf16)hi;
  return __builtin_bit_cast(u32, r);
}
DI float rbf(float x) { return __uint_as_float((u32)__builtin_bit_cast(u16, (__bf16)x) << 16); }
DI __amdgpu_buffer_rsrc_t mk_rsrc(const void* p, u32 bytes) {
  return __builtin_amdgcn_make_buffer_rsrc((void*)p, 0, bytes, 0x00020000);
}
DI void dma16(u32 lds_addr, u32 voff, __amdgpu_buffer_rsrc_t rsrc, u32 soff) {
  asm volatile("s_mov_b32 m0, %0\n\ts_nop 0\n\tbuffer_load_dwordx4 %1, %2, %3 offen lds" ::"s"(lds_addr), "v"(voff), "s"(rsrc), "s"(soff) : "memory");
}
DI void dma16u(u32 lds_addr, u32 voff, __amdgpu_buffer_rsrc_t rsrc, u32 soff) {
  lds_addr = __builtin_amdgcn_readfirstlane(lds_addr);
  soff = __builtin_amdgcn_readfirstlane(soff);
  asm volatile("s_nop 4\n\ts_mov_b32 m0, %0\n\ts_nop 0\n\tbuffer_load_dwordx4 %1, %2, %3 offen lds" ::"s"(lds_addr), "v"(voff), "s"(rsrc), "s"(soff) : "memory");
}
#define WAIT_VM(n) asm volatile("s_waitcnt vmcnt(" #n ")" ::: "memory")
#define WAIT_LGKM0() asm volatile("s_waitcnt lgkmcnt(0)" ::: "memory")
#define BAR() do { asm volatile("" ::: "memory"); __builtin_amdgcn_s_barrier(); asm volatile("" ::: "memory"); } while (0)
#define SB() __builtin_amdgcn_sched_barrier(0)
DI bf16x8 lds_frag(const LAS char* p) { return *(const LAS bf16x8*)p; }
DI bf16x8 lds_tr_frag(const LAS char* p0, const LAS char* p1) {
  s16x4 lo = __builtin_amdgcn_ds_read_tr16_b64_v4i16((LAS s16x4*)p0);
  s16x4 hi = __builtin_amdgcn_ds_read_tr16_b64_v4i16((LAS s16x4*)p1);
  s16x8 f = __builtin_shufflevector(lo, hi, 0, 1, 2, 3, 4, 5, 6, 7);
  return __builtin_bit_cast(bf16x8, f);
}
#define MFMA32(a, b, c) __builtin_amdgcn_mfma_f32_32x32x16_bf16((a), (b), (c), 0, 0, 0)
#define MFMA16(a, b, c) __builtin_amdgcn_mfma_f32_16x16x32_bf16((a), (b), (c), 0, 0, 0)
DI u64 ld64_sc1(const char* p) { return __hip_atomic_load((const GAS u64*)p, __ATOMIC_RELAXED, __HIP_MEMORY_SCOPE_AGENT); }
DI u32 ld32_sc1(const char* p) { return __hip_atomic_load((const GAS u32*)p, __ATOMIC_RELAXED, __HIP_MEMORY_SCOPE_AGENT); }
DI void st64_sc1(char* p, u64 v) { __hip_atomic_store((GAS u64*)p, v, __ATOMIC_RELAXED, __HIP_MEMORY_SCOPE_AGENT); }
DI void st32_sc1(char* p, u32 v) { __hip_atomic_store((GAS u32*)p, v, __ATOMIC_RELAXED, __HIP_MEMORY_SCOPE_AGENT); }
DI u32x4 ld128_sc1(__amdgpu_buffer_rsrc_t r, u32 voff, u32 soff) { return __builtin_amdgcn_raw_buffer_load_b128(r, voff, soff, 16); }
DI void st128_sc1(u32x4 v, __amdgpu_buffer_rsrc_t r, u32 voff, u32 soff) { __builtin_amdgcn_raw_buffer_store_b128(v, r, voff + soff, 0, 16); }
DI u64 flag_of(u32 e) { return ((u64)TAGHI << 32) | e; }


extern "C" __device__ u64 llvm_dispatch_id() __asm("llvm.amdgcn.dispatch.id");
DI u32 launch_epoch() {
  const u64 id = llvm_dispatch_id();
  const u64 dp = (u64)__builtin_amdgcn_dispatch_ptr();
  u32 h = (u32)id * 0x9E3779B1u + (u32)(id >> 32) * 0x85EBCA77u;
  h ^= (u32)(dp >> 6) * 0xC2B2AE3Du;
  h ^= (u32)(dp >> 38) * 0x27D4EB2Fu;
  return h | 1u;
}
DI void wait_flags(const char* base, int n, u32 e, int lane) {
  const u64 want = flag_of(e);
  const char* p = base + (size_t)(lane < n ? lane : 0) * 8;
_Pragma("unroll 1")
  for (u32 spins = 0; spins < SPIN_CAP; ++spins) {
    const u64 vfl = ld64_sc1(p);
    if (__all(vfl == want)) break;
    __builtin_amdgcn_s_sleep(1);
  }
  asm volatile("" ::: "memory");
}

constexpr int G1_SLOT = 24576, G1_NSLOT = 6, G1_NKB = 14;
constexpr int G1_CSUM = G1_SLOT * G1_NSLOT;
constexpr int G1_RST = G1_CSUM + 1024;
constexpr int G1_PART = G1_RST + 512;
constexpr int LD_WT = G1_PART + 2048;
constexpr int LDS_BYTES = LD_WT + 8192;
constexpr int CV_COPY = 4160;
constexpr int CV_UR = 4 * CV_COPY;
constexpr int CV_CH = CV_UR + 4608;
constexpr int CV_RED = 2 * CV_CH;

struct G1Frags { bf16x8 x[2][2]; bf16x8 g[2]; bf16x8 u[2]; };

DI bool flags4_ok(u32x4 a, u32x4 c, u32 e) {
  return a[0] == e && a[1] == TAGHI && a[2] == e && a[3] == TAGHI && c[0] == e && c[1] == TAGHI && c[2] == e && c[3] == TAGHI;
}

DI float half_sum(float v) {
  const u32 a = __float_as_uint(v);
  const auto r = __builtin_amdgcn_permlane32_swap(a, a, false, false);
  return __uint_as_float(r[0]) + __uint_as_float(r[1]);
}

DI void main_wg(const u16* x, const u16* Wg, const u16* Wu, const u16* Wd, const u16* ssm,
                const u16* Dv, char* ws, u16* out, LAS char* sm, int b) {
  int tid = threadIdx.x, lane = tid & 63;
  const int wid = __builtin_amdgcn_readfirstlane(tid >> 6);
  const int xcd = b & 7, idx = b >> 3;
  const int rp = 4 * (xcd >> 1) + (idx & 3), fc = 7 * (xcd & 1) + (idx >> 2);
  const int rh = wid >> 2, fq = wid & 3;
  int t = lane & 31, hh = lane >> 5;
  const u32 lds0 = (u32)(uintptr_t)sm;
  const u32 epoch = launch_epoch();
#define RELANE() do { tid = threadIdx.x; asm volatile("" : "+v"(tid)); lane = tid & 63; t = lane & 31; hh = lane >> 5; } while (0)
  __amdgpu_buffer_rsrc_t rws = mk_rsrc(ws, WS_END);
  __amdgpu_buffer_rsrc_t rx = mk_rsrc(x, S_ * XROW), rg = mk_rsrc(Wg, D_ * WROW), ru = mk_rsrc(Wu, D_ * WROW);
  const u32 vx = (u32)(rp * 128 + wid * 16 + (lane >> 2)) * XROW + (u32)(((lane & 3) ^ ((lane >> 4) & 3)) * 16);
  const u32 vw = (u32)(wid * 4 + (lane >> 4)) * WROW + (u32)fc * 256u + (u32)(((lane & 15) ^ (4 * (lane >> 4))) * 16);
  int kcur = (fc + rp) % 14;
#define G1_DMA1(slot, which) do { const u32 sb_ = lds0 + (u32)((slot) % G1_NSLOT) * G1_SLOT + (u32)wid * 1024u; \
    if ((which) == 0) dma16(sb_, vx, rx, (u32)kcur * 64u); else if ((which) == 1) dma16(sb_ + 8192u, vw, rg, (u32)kcur * (32u * WROW)); \
    else { dma16(sb_ + 16384u, vw, ru, (u32)kcur * (32u * WROW)); kcur = (kcur == 13) ? 0 : kcur + 1; } } while (0)
#define G1_ISSUE(slot) do { G1_DMA1(slot, 0); G1_DMA1(slot, 1); G1_DMA1(slot, 2); } while (0)
  G1_ISSUE(0); G1_ISSUE(1);
  const int wt_fb = b >> 1, wt_db0 = 7 * (b & 1) + 2 * (wid - 4);
  if (wid >= 4) {
    __amdgpu_buffer_rsrc_t rw = mk_rsrc(Wd, F_ * XROW);
    const u32 o = (u32)(wt_fb * 16 + (lane >> 2)) * XROW + (u32)(wt_db0 * 64 + (lane & 3) * 16);
    dma16u(lds0 + LD_WT + (u32)(wid - 4) * 2048u, o, rw, 0u);
    dma16u(lds0 + LD_WT + (u32)(wid - 4) * 2048u + 1024u, o + (wid < 7 ? 64u : 0u), rw, 0u);
  }
  G1_ISSUE(2); G1_ISSUE(3);
  const u32 ax0 = (u32)((rh * 64 + t) * 64 + ((hh ^ ((t >> 2) & 3)) * 16));
  const u32 ax1 = ax0 ^ 32u;
  u32 aw;
  {
    const int blk = (lane >> 4) & 1, q = (lane & 15) >> 2, p = lane & 3;
    aw = (u32)((8 * hh + q) * 256 + (((4 * fq) ^ (4 * q)) + 2 * blk + (p >> 1)) * 16 + 8 * (p & 1)) + 8192u;
  }
  f32x16 ag[2] = {}, au[2] = {};
  G1Frags fr[2];
  u64 pf_cs[7] = {};
  u32x4 pf_sp[4] = {};
  u64 pf_fp = 0;
  u32x4 pf_fa = {0, 0, 0, 0}, pf_fb = {0, 0, 0, 0};
  u32x4 hlo = {0, 0, 0, 0}, hhi = {0, 0, 0, 0};
  u32x4 sv = {0, 0, 0, 0}, c0 = {0, 0, 0, 0}, c1 = {0, 0, 0, 0}, wt0 = {0, 0, 0, 0}, wt1 = {0, 0, 0, 0};
  f32x4 ppart = {0.f, 0.f, 0.f, 0.f};
  float s1 = 0.f, s2 = 0.f, ca = 0.f, cb = 0.f;
#define STAT2(w) do { const float lo_ = bf2f_lo(w), hi_ = bf2f_hi(w); s1 += lo_; s1 += hi_; s2 = fmaf(lo_, lo_, s2); s2 = fmaf(hi_, hi_, s2); asm volatile("" : "+v"(s1), "+v"(s2)); } while (0)
#define CSUM2(w) do { ca += bf2f_lo(w); cb += bf2f_hi(w); asm volatile("" : "+v"(ca), "+v"(cb)); } while (0)
  if (wid < 4) WAIT_VM(9); else WAIT_VM(11);
  BAR();
  {
    const LAS char* sb = sm;
    fr[0].x[0][0] = lds_frag(sb + ax0); fr[0].x[1][0] = lds_frag(sb + ax0 + 2048);
    fr[0].g[0] = lds_tr_frag(sb + aw, sb + aw + 1024);
    fr[0].u[0] = lds_tr_frag(sb + 8192 + aw, sb + 8192 + aw + 1024);
    fr[0].x[0][1] = lds_frag(sb + ax1); fr[0].x[1][1] = lds_frag(sb + ax1 + 2048);
    fr[0].g[1] = lds_tr_frag(sb + aw + 4096, sb + aw + 4096 + 1024);
    fr[0].u[1] = lds_tr_frag(sb + 8192 + aw + 4096, sb + 8192 + aw + 4096 + 1024);
    sv = __builtin_bit_cast(u32x4, lds_frag(sb + ((fq >> 1) ? ax1 : ax0) + (u32)(fq & 1) * 2048u));
  }
#define SIDE(g) do { \
    if (kb == 0 && (g) >= 4) STAT2(sv[(g) - 4]); \
    if (kb == 1 && (g) == 0) { \
      const LAS char* s1_ = sm + G1_SLOT + aw + rh * 8192; \
      c0 = __builtin_bit_cast(u32x4, lds_tr_frag(s1_, s1_ + 1024)); c1 = __builtin_bit_cast(u32x4, lds_tr_frag(s1_ + 4096, s1_ + 4096 + 1024)); } \
    if (kb == 1 && (g) == 1) { s1 = half_sum(s1); s2 = half_sum(s2); } \
    if (kb == 1 && (g) == 2) { if (lane < 32) *(LAS f32x2*)(sm + G1_PART + ((rh * 64 + (fq & 1) * 32 + lane) * 2 + (fq >> 1)) * 8) = (f32x2){s1, s2}; } \
    if (kb == 1 && (g) >= 4) CSUM2(c0[(g) - 4]); \
    if (kb == 2 && (g) < 4) CSUM2(c1[g]); \
    if (kb == 2 && (g) == 0) { if (fq < 2) ppart = *(const LAS f32x4*)(sm + G1_PART + (rh * 64 + fq * 32 + t) * 16); \
      if (wid >= 4) { const int blk_ = (lane >> 4) & 1, q_ = (lane & 15) >> 2, p_ = lane & 3; \
        const LAS char* wb_ = sm + LD_WT + (wid - 4) * 2048 + (4 * hh + q_) * 64 + 32 * blk_ + 8 * p_; \
        wt0 = __builtin_bit_cast(u32x4, lds_tr_frag(wb_, wb_ + 512)); wt1 = __builtin_bit_cast(u32x4, lds_tr_frag(wb_ + 1024, wb_ + 1024 + 512)); } } \
    if (kb == 2 && (g) == 3) { \
      if (fq < 2 && lane < 32) st128_sc1((u32x4){__float_as_uint(ppart[0] + ppart[2]), epoch, __float_as_uint(ppart[1] + ppart[3]), epoch}, rws, \
                                         (u32)(rh * 64 + fq * 32 + lane) * 16u, WS_SP + (u32)(rp * 14 + fc) * 2048u); \
      if (wid >= 4) { st128_sc1(wt0, rws, (u32)lane * 16u, WS_WT + (u32)(wt_fb * 14 + wt_db0) * 1024u); \
        if (wid < 7) st128_sc1(wt1, rws, (u32)lane * 16u, WS_WT + (u32)(wt_fb * 14 + wt_db0 + 1) * 1024u); } } \
    if (kb == 3 && (g) == 0) { const float cs_ = half_sum(ca + cb); \
      if (rp < 14 && lane < 32) st64_sc1(ws + WS_CP + (size_t)((fc * 14 + rp) * 256 + rh * 128 + fq * 32 + lane) * 8, (u64)__float_as_uint(cs_) | ((u64)epoch << 32)); } \
    if (kb == 6 && (g) == 0 && tid == 0) st64_sc1(ws + WS_FW + (size_t)b * 8, flag_of(epoch));     \
    if (kb == 8 && (g) == 7) { \
      const u32 vo_ = (u32)(wid * 16 + (lane & 15)) * 16u + (u32)(lane >> 4) * 2048u; \
      _Pragma("unroll") for (int i = 0; i < 4; ++i) \
        asm volatile("buffer_load_dwordx4 %0, %1, %2, %3 offen sc1" : "=v"(pf_sp[i]) : "v"(vo_ + (i == 3 && lane >= 32 ? 0u : (u32)i * 8192u)), "s"(rws), "s"(WS_SP + (u32)rp * 14u * 2048u) : "memory"); } \
    if (kb == 9 && (g) == 7) { \
      const u32 vo_ = (u32)(wid * 32 + t) * 8u + (u32)hh * 2048u; \
      _Pragma("unroll") for (int i = 0; i < 7; ++i) \
        asm volatile("buffer_load_dwordx2 %0, %1, %2, %3 offen sc1" : "=v"(pf_cs[i]) : "v"(vo_ + (u32)i * 4096u), "s"(rws), "s"(WS_CP + (u32)fc * 14u * 2048u) : "memory"); } \
    if (kb == 11 && (g) == 7) { \
      __amdgpu_buffer_rsrc_t rh_ = mk_rsrc(ssm, 448 * 4096); \
      const u32 o_ = (u32)(2 * b + (tid >> 8)) * 4096u + (u32)(tid & 255) * 16u; \
      asm volatile("buffer_load_dwordx4 %0, %1, %2, 0 offen" : "=v"(hlo) : "v"(o_ - 16u), "s"(rh_) : "memory"); \
      asm volatile("buffer_load_dwordx4 %0, %1, %2, 0 offen" : "=v"(hhi) : "v"(o_), "s"(rh_) : "memory"); \
      if (wid == 0) { \
        asm volatile("buffer_load_dwordx2 %0, %1, %2, %3 offen sc1" : "=v"(pf_fp) : "v"((u32)t * 8u), "s"(rws), "s"(WS_FP) : "memory"); \
        asm volatile("buffer_load_dwordx4 %0, %1, %2, %3 offen sc1" : "=v"(pf_fa) : "v"((u32)lane * 32u), "s"(rws), "s"(WS_FW) : "memory"); \
        asm volatile("buffer_load_dwordx4 %0, %1, %2, %3 offen offset:16 sc1" : "=v"(pf_fb) : "v"((u32)lane * 32u), "s"(rws), "s"(WS_FW) : "memory"); } } \
  } while (0)
#pragma unroll
  for (int kb = 0; kb < G1_NKB; ++kb) {
    const bool more = kb + 1 < G1_NKB;
    if (more) {
      if (kb == 0) { if (wid < 4) WAIT_VM(6); else WAIT_VM(8); }
      else if (kb <= 8) WAIT_VM(6);
      else if (kb == 9) WAIT_VM(10);
      else if (kb == 10) WAIT_VM(17);
      else if (kb == 11) WAIT_VM(14);
      else WAIT_VM(9);
      WAIT_LGKM0();
      BAR();
    }
    G1Frags& f = fr[kb & 1];
    G1Frags& nx = fr[(kb + 1) & 1];
    const LAS char* sb = sm + ((kb + 1) % G1_NSLOT) * G1_SLOT;
    SB();
    ag[0] = MFMA32(f.g[0], f.x[0][0], ag[0]);
    SB();
    if (more) { nx.x[0][0] = lds_frag(sb + ax0); nx.g[0] = lds_tr_frag(sb + aw, sb + aw + 1024); }
    SIDE(0);
    SB();
    au[0] = MFMA32(f.u[0], f.x[0][0], au[0]);
    SB();
    if (more) { nx.u[0] = lds_tr_frag(sb + 8192 + aw, sb + 8192 + aw + 1024); nx.x[1][0] = lds_frag(sb + ax0 + 2048); }
    SIDE(1);
    SB();
    ag[1] = MFMA32(f.g[0], f.x[1][0], ag[1]);
    SB();
    if (more) { nx.x[0][1] = lds_frag(sb + ax1); nx.g[1] = lds_tr_frag(sb + aw + 4096, sb + aw + 4096 + 1024); }
    SIDE(2);
    SB();
    au[1] = MFMA32(f.u[0], f.x[1][0], au[1]);
    SB();
    if (more) { nx.u[1] = lds_tr_frag(sb + 8192 + aw + 4096, sb + 8192 + aw + 4096 + 1024); nx.x[1][1] = lds_frag(sb + ax1 + 2048); }
    SIDE(3);
    SB();
    ag[0] = MFMA32(f.g[1], f.x[0][1], ag[0]);
    SB();
    if (kb + 4 < G1_NKB) G1_DMA1(kb + 4, 0);
    SIDE(4);
    SB();
    au[0] = MFMA32(f.u[1], f.x[0][1], au[0]);
    SB();
    if (kb + 4 < G1_NKB) G1_DMA1(kb + 4, 1);
    SIDE(5);
    SB();
    ag[1] = MFMA32(f.g[1], f.x[1][1], ag[1]);
    SB();
    if (kb + 4 < G1_NKB) G1_DMA1(kb + 4, 2);
    SIDE(6);
    SB();
    au[1] = MFMA32(f.u[1], f.x[1][1], au[1]);
    SB();
    SIDE(7);
    SB();
  }
  RELANE();
  WAIT_VM(0);
  asm volatile("" : "+v"(pf_cs[0]), "+v"(pf_cs[1]), "+v"(pf_cs[2]), "+v"(pf_cs[3]), "+v"(pf_cs[4]), "+v"(pf_cs[5]), "+v"(pf_cs[6]),
                    "+v"(pf_sp[0]), "+v"(pf_sp[1]), "+v"(pf_sp[2]), "+v"(pf_sp[3]), "+v"(hlo), "+v"(hhi), "+v"(pf_fp), "+v"(pf_fa), "+v"(pf_fb));
  {
    const u32 vs_ = (u32)(wid * 16 + (lane & 15)) * 16u + (u32)(lane >> 4) * 2048u;
    const u32 vc_ = (u32)(wid * 32 + t) * 8u + (u32)hh * 2048u;
_Pragma("unroll 1")
    for (u32 spins = 0; spins < SPIN_CAP; ++spins) {
      bool ok = true;
#pragma unroll
      for (int i = 0; i < 4; ++i) ok = ok && pf_sp[i][1] == epoch && pf_sp[i][3] == epoch;
#pragma unroll
      for (int i = 0; i < 7; ++i) ok = ok && (u32)(pf_cs[i] >> 32) == epoch;
      if (__all(ok)) break;
      __builtin_amdgcn_s_sleep(1);
#pragma unroll
      for (int i = 0; i < 4; ++i) pf_sp[i] = ld128_sc1(rws, vs_ + (i == 3 && lane >= 32 ? 0u : (u32)i * 8192u), WS_SP + (u32)rp * 14u * 2048u);
#pragma unroll
      for (int i = 0; i < 7; ++i) pf_cs[i] = ld64_sc1(ws + WS_CP + (size_t)fc * 14 * 2048 + vc_ + (size_t)i * 4096);
    }
    float t1 = __uint_as_float(pf_sp[0][0]) + __uint_as_float(pf_sp[1][0]) + __uint_as_float(pf_sp[2][0]);
    float t2 = __uint_as_float(pf_sp[0][2]) + __uint_as_float(pf_sp[1][2]) + __uint_as_float(pf_sp[2][2]);
    if (lane < 32) { t1 += __uint_as_float(pf_sp[3][0]); t2 += __uint_as_float(pf_sp[3][2]); }
    t1 = half_sum(t1); t2 = half_sum(t2);
    t1 += __shfl_xor(t1, 16); t2 += __shfl_xor(t2, 16);
    const float mean = t1 * (1.0f / D_);
    const float var = fmaxf(t2 * (1.0f / D_) - mean * mean, 0.f);
    const float den = rbf(__builtin_amdgcn_sqrtf(rbf(rbf(var) + 1e-5f)));
    const u32 sw = pack_bf16(mean, den);
    if (lane < 16) {
      *(LAS u32*)(sm + G1_RST + (wid * 16 + lane) * 4) = sw;
      if (fc == 0) st64_sc1(ws + WS_ST + (size_t)(rp * 128 + wid * 16 + lane) * 8, (u64)sw | ((u64)epoch << 32));
    }
    float cs = 0.f;
#pragma unroll
    for (int i = 0; i < 7; ++i) cs += __uint_as_float((u32)pf_cs[i]);
    cs = half_sum(cs);
    if (lane < 32) *(LAS float*)(sm + G1_CSUM + (wid * 32 + lane) * 4) = cs;
  }
  if (wid == 0) {
    if (!__all(pf_fp == flag_of(epoch))) wait_flags(ws + WS_FP, 32, epoch, lane);
    bool ok = lane >= 56 || flags4_ok(pf_fa, pf_fb, epoch);
_Pragma("unroll 1")
    for (u32 spins = 0; !__all(ok) && spins < SPIN_CAP; ++spins) {
      __builtin_amdgcn_s_sleep(2);
      const u32x4 a = ld128_sc1(rws, (u32)lane * 32u, WS_FW), c = ld128_sc1(rws, (u32)lane * 32u + 16u, WS_FW);
      ok = lane >= 56 || flags4_ok(a, c, epoch);
    }
  }
  WAIT_LGKM0();
  BAR();
  const int tp = 4 * xcd + (idx & 3), dc = idx >> 2;
  const u32 wo = WS_WT + (u32)(wid * 14 * 14 + 2 * dc) * 1024u;
  bf16x8 wf[14][2];
#define W_LOAD(li) wf[(li) >> 1][(li) & 1] = __builtin_bit_cast(bf16x8, ld128_sc1(rws, (u32)lane * 16u, wo + (u32)(((li) >> 1) * 14 + ((li) & 1)) * 1024u))
  const u32x4 kvv = ld128_sc1(rws, (u32)(tid & 255) * 16u, WS_KVR + (u32)(2 * b + (wid >> 2)) * 4096u);
  {
    f32x4 cg[4], cu[4];
    const LAS float* csum = (const LAS float*)(sm + G1_CSUM) + fq * 32;
#pragma unroll
    for (int g4 = 0; g4 < 4; ++g4) { cg[g4] = *(const LAS f32x4*)(csum + 8 * g4 + 4 * hh); cu[g4] = *(const LAS f32x4*)(csum + 128 + 8 * g4 + 4 * hh); }
#pragma unroll
    for (int tb = 0; tb < 2; ++tb) {
      const u32 sw = *(const LAS u32*)(sm + G1_RST + (rh * 64 + tb * 32 + t) * 4);
      const float nmb = -bf2f_lo(sw);
      u32 hv[8];
#pragma unroll
      for (int r = 0; r < 16; r += 2) {
        f32x2 g2 = (f32x2){cg[r >> 2][r & 3], cg[r >> 2][(r & 3) + 1]} * nmb + (f32x2){ag[tb][r], ag[tb][r + 1]};
        f32x2 u2 = (f32x2){cu[r >> 2][r & 3], cu[r >> 2][(r & 3) + 1]} * nmb + (f32x2){au[tb][r], au[tb][r + 1]};
        g2[0] = fmaxf(g2[0], 0.f); g2[1] = fmaxf(g2[1], 0.f);
        g2 = g2 * u2;
        hv[r >> 1] = pack_bf16(g2[0], g2[1]);
      }
      const int tbg = rp * 4 + rh * 2 + tb, fbg = fc * 8 + fq * 2;
      const u32 ho = WS_H + (u32)(tbg * 112 + fbg) * 1024u;
      st128_sc1((u32x4){hv[0], hv[1], hv[2], hv[3]}, rws, (u32)lane * 16u, ho);
      st128_sc1((u32x4){hv[4], hv[5], hv[6], hv[7]}, rws, (u32)lane * 16u, ho + 1024u);
    }
  }
  SB();
  W_LOAD(0); W_LOAD(1); W_LOAD(2); W_LOAD(3); W_LOAD(4); W_LOAD(5);
  SB();
  {
    const int ch = tid >> 8, c = (tid & 255) + 1;
    u32x4 lo = hlo, hi = hhi;
    if (c < 2) lo = (u32x4){0, 0, 0, 0};
    LAS char* cb_ = sm + ch * CV_CH + c * 16;
    {
      const u32 a01 = __builtin_amdgcn_alignbit(lo[1], lo[0], 16), a12 = __builtin_amdgcn_alignbit(lo[2], lo[1], 16),
                a23 = __builtin_amdgcn_alignbit(lo[3], lo[2], 16), a34 = __builtin_amdgcn_alignbit(hi[0], lo[3], 16),
                a45 = __builtin_amdgcn_alignbit(hi[1], hi[0], 16);
      *(LAS u32x4*)(cb_) = (u32x4){a01, a12, a23, a34};
      *(LAS u32x4*)(cb_ + CV_COPY) = (u32x4){lo[1], lo[2], lo[3], hi[0]};
      *(LAS u32x4*)(cb_ + 2 * CV_COPY) = (u32x4){a12, a23, a34, a45};
      *(LAS u32x4*)(cb_ + 3 * CV_COPY) = (u32x4){lo[2], lo[3], hi[0], hi[1]};
    }
    if (c == 256) {
      const u32 a01 = __builtin_amdgcn_alignbit(hi[1], hi[0], 16), a12 = __builtin_amdgcn_alignbit(hi[2], hi[1], 16),
                a23 = __builtin_amdgcn_alignbit(hi[3], hi[2], 16), a34 = hi[3] >> 16;
      *(LAS u32x4*)(cb_ + 16) = (u32x4){a01, a12, a23, a34};
      *(LAS u32x4*)(cb_ + 16 + CV_COPY) = (u32x4){hi[1], hi[2], hi[3], 0u};
      *(LAS u32x4*)(cb_ + 16 + 2 * CV_COPY) = (u32x4){a12, a23, a34, 0u};
      *(LAS u32x4*)(cb_ + 16 + 3 * CV_COPY) = (u32x4){hi[2], hi[3], 0u, 0u};
    }
    if (c <= 4) *(LAS u32x4*)(sm + ch * CV_CH + (c - 1) * CV_COPY) = (u32x4){0, 0, 0, 0};
    if (c <= 32) *(LAS u32x4*)(sm + ch * CV_CH + CV_UR + 4096 + (c - 1) * 16) = (u32x4){0, 0, 0, 0};
    *(LAS u32x4*)(sm + ch * CV_CH + CV_UR + (tid & 255) * 16) = kvv;
  }
  WAIT_LGKM0();
  BAR();
  RELANE();
  u64 pf_fh = 0;
  {
    const int ch = wid >> 2, pw = wid & 3;
    const int bb = lane & 15, g = lane >> 4;
    const LAS char* ca_ = sm + ch * CV_CH + (bb & 3) * CV_COPY + 2 * (8 * g + (bb & 12)) + 128 * pw;
    const LAS char* cbp = sm + ch * CV_CH + CV_UR + 2 * (2032 - 16 * bb + 8 * g) + 128 * pw;
    f32x4 acc[8] = {};
    bf16x8 A[2][8], B[2][8];
#pragma unroll
    for (int i = 0; i < 8; ++i) {
      u32x2 lo = *(const LAS u32x2*)(ca_ + 512 * i), hi = *(const LAS u32x2*)(ca_ + 512 * i + 8);
      A[0][i] = __builtin_bit_cast(bf16x8, (u32x4){lo[0], lo[1], hi[0], hi[1]});
      B[0][i] = *(const LAS bf16x8*)(cbp - 512 * i);
    }
#pragma unroll
    for (int pp = 0; pp < 2; ++pp) {
#pragma unroll
      for (int nt = 0; nt < 8; ++nt) {
#pragma unroll
        for (int i = 0; i <= nt; ++i) {
          acc[nt] = MFMA16(A[pp][i], B[pp][nt - i], acc[nt]);
          const int m = pp * 36 + nt * (nt + 1) / 2 + i;
          if (m % 3 == 0 && 6 + m / 3 < 28) { SB(); W_LOAD(6 + m / 3); SB(); }
          if (m == 23) {
            SB();
            WAIT_VM(14);
            BAR();
            if (tid == 0) st64_sc1(ws + WS_FH + (size_t)(rp * 16 + fc) * 8, flag_of(epoch));
            SB();
          }
          if (m == 69 && wid == 0) {
            SB();
            asm volatile("buffer_load_dwordx2 %0, %1, %2, %3 offen sc1" : "=v"(pf_fh) : "v"((u32)(lane < 14 ? lane : 0) * 8u), "s"(rws), "s"(WS_FH + (u32)(tp >> 1) * 128u) : "memory");
            SB();
          }
        }
        if (pp == 0) {
          u32x2 lo = *(const LAS u32x2*)(ca_ + 64 + 512 * nt), hi = *(const LAS u32x2*)(ca_ + 64 + 512 * nt + 8);
          A[1][nt] = __builtin_bit_cast(bf16x8, (u32x4){lo[0], lo[1], hi[0], hi[1]});
          B[1][nt] = *(const LAS bf16x8*)(cbp + 64 - 512 * nt);
        }
      }
    }
    LAS char* red = sm + CV_RED + ch * 32768;
#pragma unroll
    for (int nt = 0; nt < 8; ++nt)
      if ((nt >> 1) != pw) *(LAS f32x4*)(red + pw * 8192 + nt * 1024 + lane * 16) = acc[nt];
    const int d = 2 * b + ch;
    const float Dd = bf2f_lo((u32)Dv[d]);
    WAIT_LGKM0();
    BAR();
#pragma unroll
    for (int e = 0; e < 2; ++e) {
      const int nt = 2 * pw + e;
      f32x4 y = (pw == 0) ? acc[e] : (pw == 1) ? acc[2 + e] : (pw == 2) ? acc[4 + e] : acc[6 + e];
#pragma unroll
      for (int src = 0; src < 4; ++src)
        if (src != pw) y += *(const LAS f32x4*)(red + src * 8192 + nt * 1024 + lane * 16);
      const int t0 = 256 * nt + 16 * bb + 4 * g;
      u32x2 kvp = *(const LAS u32x2*)(sm + ch * CV_CH + CV_UR + 2 * (2044 - t0));
      const float k3 = bf2f_lo(kvp[0]), k2 = bf2f_hi(kvp[0]), k1 = bf2f_lo(kvp[1]), k0 = bf2f_hi(kvp[1]);
      const float y0 = rbf(y[0]) + rbf(k0 * Dd), y1 = rbf(y[1]) + rbf(k1 * Dd), y2 = rbf(y[2]) + rbf(k2 * Dd), y3 = rbf(y[3]) + rbf(k3 * Dd);
      st64_sc1(ws + WS_YT + (size_t)d * 4096 + (size_t)t0 * 2, (u64)pack_bf16(y0, y1) | ((u64)pack_bf16(y2, y3) << 32));
    }
  }
  if (wid == 0) {
    WAIT_VM(2);
    asm volatile("" : "+v"(pf_fh));
    if (!__all(pf_fh == flag_of(epoch))) wait_flags(ws + WS_FH + (size_t)(tp >> 1) * 128, 14, epoch, lane);
  }
  WAIT_LGKM0();
  BAR();
  RELANE();
  u64 pf_fy = 0;
  f32x16 acc[2][2] = {};
  const int db_o = wid >> 2, tb_o = (wid >> 1) & 1, hf_o = wid & 1;
  const int te = tp * 64 + tb_o * 32 + t;
  const int d0 = dc * 64 + db_o * 32 + hf_o * 16 + 4 * hh;
  u32x2 xv[2];
  u64 stv;
  u32 yw[8];
  {
    const u32 hb = WS_H + (u32)((2 * tp) * 112 + wid * 14) * 1024u;
    bf16x8 hf[2][14];
#pragma unroll
    for (int i = 0; i < 14; ++i) hf[0][i] = __builtin_bit_cast(bf16x8, ld128_sc1(rws, (u32)lane * 16u, hb + (u32)i * 1024u));
#pragma unroll
    for (int i = 0; i < 2; ++i) hf[1][i] = __builtin_bit_cast(bf16x8, ld128_sc1(rws, (u32)lane * 16u, hb + (u32)(112 + i) * 1024u));
#pragma unroll
    for (int i = 0; i < 14; ++i) {
      acc[0][0] = MFMA32(wf[i][0], hf[0][i], acc[0][0]);
      acc[1][0] = MFMA32(wf[i][1], hf[0][i], acc[1][0]);
      SB();
      if (i + 2 < 14) hf[1][i + 2] = __builtin_bit_cast(bf16x8, ld128_sc1(rws, (u32)lane * 16u, hb + (u32)(112 + i + 2) * 1024u));
      if (i == 1) {
        WAIT_VM(18);
        BAR();
        if (tid == 0) st64_sc1(ws + WS_FY + (size_t)b * 8, flag_of(epoch));
      }
      if (i == 12) {
        xv[0] = *(const u32x2*)(x + (size_t)te * D_ + d0);
        xv[1] = *(const u32x2*)(x + (size_t)te * D_ + d0 + 8);
        stv = ld64_sc1(ws + WS_ST + (size_t)te * 8);
      }
      SB();
    }
#pragma unroll
    for (int db = 0; db < 2; ++db)
#pragma unroll
      for (int h2 = 0; h2 < 2; ++h2) {
        const int o = (db << 2) | h2;
        LAS char* pz = sm + o * 16384 + wid * 2048 + lane * 16;
        *(LAS f32x4*)pz = (f32x4){acc[db][0][8 * h2], acc[db][0][8 * h2 + 1], acc[db][0][8 * h2 + 2], acc[db][0][8 * h2 + 3]};
        *(LAS f32x4*)(pz + 1024) = (f32x4){acc[db][0][8 * h2 + 4], acc[db][0][8 * h2 + 5], acc[db][0][8 * h2 + 6], acc[db][0][8 * h2 + 7]};
      }
    if (wid == 0) pf_fy = ld64_sc1(ws + WS_FY + (size_t)(dc * 32 + t) * 8);
#pragma unroll
    for (int i = 0; i < 9; ++i) {
      acc[0][1] = MFMA32(wf[i][0], hf[1][i], acc[0][1]);
      acc[1][1] = MFMA32(wf[i][1], hf[1][i], acc[1][1]);
    }
    if (wid == 0 && !__all(pf_fy == flag_of(epoch))) wait_flags(ws + WS_FY + (size_t)dc * 256, 32, epoch, lane);
    BAR();
#pragma unroll
    for (int j = 0; j < 8; ++j) yw[j] = ld32_sc1(ws + WS_YT + (size_t)(d0 + (j & 3) + 8 * (j >> 2)) * 4096 + (size_t)(te & ~1) * 2);
#pragma unroll
    for (int i = 9; i < 14; ++i) {
      acc[0][1] = MFMA32(wf[i][0], hf[1][i], acc[0][1]);
      acc[1][1] = MFMA32(wf[i][1], hf[1][i], acc[1][1]);
    }
  }
#pragma unroll
  for (int db = 0; db < 2; ++db)
#pragma unroll
    for (int h2 = 0; h2 < 2; ++h2) {
      const int o = (db << 2) | 2 | h2;
      LAS char* pz = sm + o * 16384 + wid * 2048 + lane * 16;
      *(LAS f32x4*)pz = (f32x4){acc[db][1][8 * h2], acc[db][1][8 * h2 + 1], acc[db][1][8 * h2 + 2], acc[db][1][8 * h2 + 3]};
      *(LAS f32x4*)(pz + 1024) = (f32x4){acc[db][1][8 * h2 + 4], acc[db][1][8 * h2 + 5], acc[db][1][8 * h2 + 6], acc[db][1][8 * h2 + 7]};
    }
_Pragma("unroll 1")
  for (u32 spins = 0; !__all((u32)(stv >> 32) == epoch) && spins < SPIN_CAP; ++spins) stv = ld64_sc1(ws + WS_ST + (size_t)te * 8);
  WAIT_LGKM0();
  BAR();
  f32x4 o0 = {0.f, 0.f, 0.f, 0.f}, o1 = {0.f, 0.f, 0.f, 0.f};
#pragma unroll
  for (int src = 0; src < 8; ++src) {
    const LAS char* pz = sm + wid * 16384 + src * 2048 + lane * 16;
    o0 += *(const LAS f32x4*)pz;
    o1 += *(const LAS f32x4*)(pz + 1024);
  }
  {
    const float mb = bf2f_lo((u32)stv), rs = __builtin_amdgcn_rcpf(bf2f_hi((u32)stv));
    const float r2 = rs * rs;
#pragma unroll
    for (int e = 0; e < 2; ++e) {
      const f32x4 ff = e ? o1 : o0;
      const float xf[4] = {bf2f_lo(xv[e][0]), bf2f_hi(xv[e][0]), bf2f_lo(xv[e][1]), bf2f_hi(xv[e][1])};
      float res[4];
#pragma unroll
      for (int j = 0; j < 4; ++j) {
        const u32 w = yw[4 * e + j];
        const float y = (te & 1) ? bf2f_hi(w) : bf2f_lo(w);
        const float xn = rbf(rbf(xf[j] - mb) * rs);
        const float sv_ = rbf(xn * y);
        const float a = rbf(ff[j] * r2);
        const float gl = rbf(a * __builtin_amdgcn_rcpf(1.0f + __expf(-a)));
        res[j] = xf[j] + rbf(sv_ * gl);
      }
      *(u32x2*)(out + (size_t)te * D_ + d0 + 8 * e) = (u32x2){pack_bf16(res[0], res[1]), pack_bf16(res[2], res[3])};
    }
  }
}

DI void aux_wg(const u16* k, const u16* v, const u16* sk, char* ws, LAS char* sm, int b) {
  const int tid = threadIdx.x, lane = tid & 63, wid = __builtin_amdgcn_readfirstlane(tid >> 6);
  const int s = b - 224;
  const u32 epoch = launch_epoch();
  __amdgpu_buffer_rsrc_t rws = mk_rsrc(ws, WS_END);
  u32x4 kr[11], vr[8], wq[4];
  const int cgp = lane & 7, tg = lane >> 3;
  const int c0 = wid * 64 + cgp * 8, t0 = s * 64 + tg * 8;
  if (wid < 7) {
    __amdgpu_buffer_rsrc_t rk = mk_rsrc(k, S_ * XROW), rv = mk_rsrc(v, S_ * XROW);
#pragma unroll
    for (int i = 0; i < 4; ++i) wq[i] = *(const u32x4*)(sk + c0 * 4 + i * 8);
#pragma unroll
    for (int i = 0; i < 11; ++i) kr[i] = __builtin_amdgcn_raw_buffer_load_b128(rk, (u32)((t0 - 3 + i) * XROW + c0 * 2), 0, 0);
#pragma unroll
    for (int i = 0; i < 8; ++i) vr[i] = __builtin_amdgcn_raw_buffer_load_b128(rv, (u32)((t0 + i) * XROW + c0 * 2), 0, 0);
  }
  if (wid < 7) {
#pragma unroll
    for (int c = 0; c < 8; ++c) {
      const u32 wa = wq[c >> 1][(c & 1) * 2], wb = wq[c >> 1][(c & 1) * 2 + 1];
      const float w0 = bf2f_lo(wa), w1 = bf2f_hi(wa), w2 = bf2f_lo(wb), w3 = bf2f_hi(wb);
      float kf[11];
#pragma unroll
      for (int i = 0; i < 11; ++i) kf[i] = (c & 1) ? bf2f_hi(kr[i][c >> 1]) : bf2f_lo(kr[i][c >> 1]);
      float o[8];
#pragma unroll
      for (int i = 0; i < 8; ++i) {
        float ks = kf[i] * w3;
        ks = fmaf(kf[i + 1], w2, ks);
        ks = fmaf(kf[i + 2], w1, ks);
        ks = fmaf(kf[i + 3], w0, ks);
        const float vf = (c & 1) ? bf2f_hi(vr[i][c >> 1]) : bf2f_lo(vr[i][c >> 1]);
        o[i] = rbf(ks) * vf;
      }
      u32x4 ov = (u32x4){pack_bf16(o[7], o[6]), pack_bf16(o[5], o[4]), pack_bf16(o[3], o[2]), pack_bf16(o[1], o[0])};
      st128_sc1(ov, rws, (u32)(c0 + c) * 4096u + (u32)(2040 - t0) * 2u, WS_KVR);
    }
  }
  WAIT_VM(0);
  BAR();
  if (tid == 0) st64_sc1(ws + WS_FP + (size_t)s * 8, flag_of(epoch));
}

__global__ __launch_bounds__(512, 2) void fused_k(const u16* __restrict__ x, const u16* __restrict__ k, const u16* __restrict__ v,
                                                  const u16* __restrict__ sk, const u16* __restrict__ ssm, const u16* __restrict__ Dv,
                                                  const u16* __restrict__ Wg, const u16* __restrict__ Wu, const u16* __restrict__ Wd,
                                                  char* __restrict__ ws, u16* __restrict__ out) {
  __shared__ __attribute__((aligned(16))) char smem[LDS_BYTES];
  LAS char* sm = (LAS char*)smem;
  const int b = blockIdx.x;
  if (b < 224) main_wg(x, Wg, Wu, Wd, ssm, Dv, ws, out, sm, b);
  else aux_wg(k, v, sk, ws, sm, b);
}

extern "C" void kernel_launch(void* const* d_in, const int* in_sizes, int n_in, void* d_out, int out_size, void* d_ws, size_t ws_size,
                              hipStream_t stream) {
  fused_k<<<256, 512, 0, stream>>>((const u16*)d_in[0], (const u16*)d_in[1], (const u16*)d_in[2], (const u16*)d_in[3], (const u16*)d_in[4],
                                   (const u16*)d_in[5], (const u16*)d_in[7], (const u16*)d_in[8], (const u16*)d_in[9], (char*)d_ws, (u16*)d_out);
}
```
